```python
import torch

def _softmax(x, axis=-1):
    return torch.softmax(x, dim=axis)
def _gelu(x):
    return torch.nn.functional.gelu(x, approximate="tanh")
def _silu(x):
    return torch.nn.functional.silu(x)
def _sigmoid(x):
    return torch.sigmoid(x)
def _layernorm(x, axis=-1, eps=1e-5):
    m = x.mean(dim=axis, keepdim=True)
    v = x.var(dim=axis, keepdim=True, unbiased=False)
    return (x - m) / torch.sqrt(v + eps)
def _scatter_add(tgt, idx, src, axis=0):
    return tgt.clone().index_add_(axis, idx, src)
def _segment_sum(src, seg, n):
    out = torch.zeros((n,) + src.shape[1:], dtype=src.dtype)
    return out.index_add_(0, seg, src)
def _pad(x, pw):
    flat = tuple(v for pair in reversed(pw) for v in pair)
    return torch.nn.functional.pad(x, flat)

kernel_name = 'sequential_preLN__sliding_block_cached__dense_activation_relu__decode'

def setup_inputs():

    _w = (lambda a: (lambda t: t.to(torch.bfloat16) if t.is_floating_point() else t)(torch.as_tensor(a)))
    def _A_setup():
        B, Sq, D = 8, 1, 576
        Hq, Hkv, Sk, Dh = 32, 16, 8192, 128
        assert Hq % Hkv == 0, (Hq, Hkv)
        return {
            "x": _w((lambda *s: torch.randn(*s, dtype=torch.bfloat16) * 0.05)(B, Sq, D)),
            "k_cache": _w((lambda *s: torch.randn(*s, dtype=torch.bfloat16) * 3.2)(B, Hkv, Sk, Dh)),
            "v_cache": _w((lambda *s: torch.randn(*s, dtype=torch.bfloat16) * 0.4)(B, Hkv, Sk, Dh)),
            "wq": _w((lambda *s: torch.randn(*s, dtype=torch.bfloat16) * 3.2)(D, Hq * Dh) * (D ** -0.5)),
            "wo": _w((lambda *s: torch.randn(*s, dtype=torch.bfloat16) * 0.4)(Hq * Dh, D) * ((Hq * Dh) ** -0.5)),
        }
    _A_in = _A_setup()

    def _B_setup():
        B, S, D, Dff = 8, 1, 576, 4608
        return {
            "x": _w((lambda *s: torch.randn(*s, dtype=torch.bfloat16) * 0.05)(B, S, D)),
            "w1": _w((lambda *s: torch.randn(*s, dtype=torch.bfloat16) * 0.05)(D, Dff)),
            "w2": _w((lambda *s: torch.randn(*s, dtype=torch.bfloat16) * 0.05)(Dff, D)),
        }
    _B_in = _B_setup()

    return {
        "A_x": _A_in["x"],
        "A_k_cache": _A_in["k_cache"],
        "A_v_cache": _A_in["v_cache"],
        "A_wq": _A_in["wq"],
        "A_wo": _A_in["wo"],
        "B_x": _B_in["x"],
        "B_w1": _B_in["w1"],
        "B_w2": _B_in["w2"],
    }

def reference(A_x, A_k_cache, A_v_cache, A_wq, A_wo, B_x, B_w1, B_w2):
    def _A_ref(x, k_cache, v_cache, wq, wo):
        B, Sq, D = x.shape
        Hq, Dh = 32, 128
        Hkv, Sk = k_cache.shape[1], k_cache.shape[2]
        W = min(4096, max(256, Sk // 4))
        rep = Hq // Hkv
        q = (lambda x, *d: x.permute(*d))((x @ wq).reshape(B, Sq, Hq, Dh), 0, 2, 1, 3)
        k = (lambda x, n, axis: torch.repeat_interleave(x, n, dim=axis))(k_cache[:, :, Sk - W:, :], rep, axis=1)
        v = (lambda x, n, axis: torch.repeat_interleave(x, n, dim=axis))(v_cache[:, :, Sk - W:, :], rep, axis=1)
        scores = torch.einsum("bhqd,bhkd->bhqk", q, k) * (Dh ** -0.5)
        p = _softmax(scores, axis=-1)
        o = torch.einsum("bhqk,bhkd->bhqd", p, v)
        return (lambda x, *d: x.permute(*d))(o, 0, 2, 1, 3).reshape(B, Sq, Hq * Dh) @ wo

    def _B_ref(x, w1, w2):
        h = torch.einsum("bsd,df->bsf", x, w1)
        zero = torch.zeros_like(h)
        h = torch.maximum(h, zero)
        return torch.einsum("bsf,fd->bsd", h, w2)

    x = A_x
    h = x + _A_ref(_layernorm(x, axis=-1), A_k_cache, A_v_cache, A_wq, A_wo)
    h2 = h + _B_ref(_layernorm(h, axis=-1), B_w1, B_w2)
    return h2
```

```cpp
#include <hip/hip_runtime.h>
#include <hip/hip_bf16.h>
#define MIMIC 0
typedef unsigned short u16;
constexpr int B = 8, D = 576, HQ = 32, HKV = 16, SK = 8192, DH = 128, W = 2048, DFF = 4608;
__device__ __forceinline__ float bf2f(u16 v) { return __uint_as_float(((unsigned)v) << 16); }
__device__ __forceinline__ u16 f2bf(float f) { unsigned u = __float_as_uint(f); u += 0x7FFFu + ((u >> 16) & 1u); return (u16)(u >> 16); }
__device__ __forceinline__ float rb(float f) { return MIMIC ? bf2f(f2bf(f)) : f; }

__device__ void ln_row(const float* xin, float* xout, int lane) {
  float s = 0.f;
  for (int i = lane; i < D; i += 64) s += xin[i];
  for (int o = 32; o; o >>= 1) s += __shfl_xor(s, o);
  float mean = s / D;
  float v = 0.f;
  for (int i = lane; i < D; i += 64) { float d = xin[i] - mean; v += d * d; }
  for (int o = 32; o; o >>= 1) v += __shfl_xor(v, o);
  float var = rb(v / D); float m = rb(mean);
  float den = rb(sqrtf(rb(var + 1e-5f)));
  for (int i = lane; i < D; i += 64) xout[i] = rb(rb(xin[i] - m) / den);
}
__global__ void k_ln_gemv(const u16* xb, const float* xf, const u16* w, float* out, int N, int relu) {
  __shared__ float xs[B][D]; __shared__ float xn[B][D]; __shared__ float red[8][B][64];
  int tid = threadIdx.x, lane = tid & 63, wv = tid >> 6;
  for (int i = tid; i < B * D; i += 512) (&xs[0][0])[i] = xb ? bf2f(xb[i]) : xf[i];
  __syncthreads();
  ln_row(xs[wv], xn[wv], lane);
  __syncthreads();
  int n = blockIdx.x * 64 + lane;
  float acc[B] = {0};
  for (int k = wv; k < D; k += 8) { float ww = bf2f(w[(size_t)k * N + n]);
    for (int b = 0; b < B; ++b) acc[b] += xn[b][k] * ww; }
  for (int b = 0; b < B; ++b) red[wv][b][lane] = acc[b];
  __syncthreads();
  float s = 0.f; for (int j = 0; j < 8; ++j) s += red[j][wv][lane];
  s = rb(s); if (relu) s = fmaxf(s, 0.f);
  out[(size_t)wv * N + n] = s;
}
__global__ void k_attn(const float* q, const u16* kc, const u16* vc, float* o) {
  __shared__ float sc[W]; __shared__ float qs[DH]; __shared__ float red[16]; __shared__ float oacc[4][DH];
  int b = blockIdx.x / HQ, h = blockIdx.x % HQ, kvh = h / 2, tid = threadIdx.x;
  if (tid < DH) qs[tid] = q[(size_t)b * HQ * DH + h * DH + tid];
  __syncthreads();
  const u16* kb = kc + ((size_t)(b * HKV + kvh) * SK + (SK - W)) * DH;
  const u16* vb = vc + ((size_t)(b * HKV + kvh) * SK + (SK - W)) * DH;
  float mx = -INFINITY;
  for (int j = tid; j < W; j += 512) { float s = 0.f; for (int d = 0; d < DH; ++d) s += qs[d] * bf2f(kb[(size_t)j * DH + d]);
    s = rb(rb(s) * 0.08838834764831845f); sc[j] = s; mx = fmaxf(mx, s); }
  for (int o2 = 32; o2; o2 >>= 1) mx = fmaxf(mx, __shfl_xor(mx, o2));
  if ((tid & 63) == 0) red[tid >> 6] = mx;
  __syncthreads();
  mx = red[0]; for (int j = 1; j < 8; ++j) mx = fmaxf(mx, red[j]);
  float sum = 0.f;
  for (int j = tid; j < W; j += 512) { float e = expf(sc[j] - mx); sc[j] = e; sum += e; }
  for (int o2 = 32; o2; o2 >>= 1) sum += __shfl_xor(sum, o2);
  if ((tid & 63) == 0) red[8 + (tid >> 6)] = sum;
  __syncthreads();
  sum = 0.f; for (int j = 0; j < 8; ++j) sum += red[8 + j];
  int d = tid & 127, g = tid >> 7; float a = 0.f;
  for (int j = g; j < W; j += 4) a += rb(sc[j] / sum) * bf2f(vb[(size_t)j * DH + d]);
  oacc[g][d] = a;
  __syncthreads();
  if (tid < DH) o[(size_t)b * HQ * DH + h * DH + tid] = rb(oacc[0][tid] + oacc[1][tid] + oacc[2][tid] + oacc[3][tid]);
}
__global__ void k_gemv_res(const float* in, const u16* w, int K, const u16* resb, const float* resf, float* outf, u16* outb) {
  __shared__ float red[16][B][64];
  int tid = threadIdx.x, lane = tid & 63, wv = tid >> 6, n = blockIdx.x * 64 + lane;
  float acc[B] = {0};
  for (int k = wv; k < K; k += 16) { float ww = bf2f(w[(size_t)k * D + n]);
    for (int b = 0; b < B; ++b) acc[b] += in[(size_t)b * K + k] * ww; }
  for (int b = 0; b < B; ++b) red[wv][b][lane] = acc[b];
  __syncthreads();
  if (wv < B) { float s = 0.f; for (int j = 0; j < 16; ++j) s += red[j][wv][lane];
    float r = resb ? bf2f(resb[wv * D + n]) : resf[wv * D + n];
    float v = rb(r + rb(s));
    if (outf) outf[wv * D + n] = v; if (outb) outb[wv * D + n] = f2bf(v); }
}
extern "C" void kernel_launch(void* const* d_in, const int* in_sizes, int n_in, void* d_out, int out_size, void* d_ws, size_t ws_size, hipStream_t stream) {
  const u16* x = (const u16*)d_in[0]; const u16* kc = (const u16*)d_in[1]; const u16* vc = (const u16*)d_in[2];
  const u16* wq = (const u16*)d_in[3]; const u16* wo = (const u16*)d_in[4]; const u16* w1 = (const u16*)d_in[6]; const u16* w2 = (const u16*)d_in[7];
  float* q = (float*)d_ws; float* o = q + B * 4096; float* h = o + B * 4096; float* u = h + B * D;
  k_ln_gemv<<<64, 512, 0, stream>>>(x, nullptr, wq, q, 4096, 0);
  k_attn<<<B * HQ, 512, 0, stream>>>(q, kc, vc, o);
  k_gemv_res<<<9, 1024, 0, stream>>>(o, wo, 4096, x, nullptr, h, nullptr);
  k_ln_gemv<<<72, 512, 0, stream>>>(nullptr, h, w1, u, DFF, 1);
  k_gemv_res<<<9, 1024, 0, stream>>>(u, w2, DFF, nullptr, h, nullptr, (u16*)d_out);
}
```

```cpp
#include <hip/hip_runtime.h>

typedef unsigned short u16;
typedef unsigned long long u64;
typedef unsigned v4u __attribute__((ext_vector_type(4)));
typedef unsigned v2u __attribute__((ext_vector_type(2)));
typedef float f32x4 __attribute__((ext_vector_type(4)));
typedef float f32x2 __attribute__((ext_vector_type(2)));
typedef __bf16 bf16x8 __attribute__((ext_vector_type(8)));
typedef __bf16 bf16x2 __attribute__((ext_vector_type(2)));
typedef short s16x4 __attribute__((ext_vector_type(4)));
#define LAS __attribute__((address_space(3)))

constexpr int TOTB = 280;
constexpr int PACE = 6;
constexpr int QS = 46;
constexpr int MSTEP = 48;
constexpr int QDELAY = 80, RDELAY = 20;
constexpr int YDELAY = 42;
constexpr int KPUB = 4;

constexpr int D = 576, HKV = 16, SK = 8192, WIN = 2048, DFF = 4608, NQ = 4096;
constexpr int AUX_NT = 2, AUX_SC1 = 16;
constexpr unsigned OOB = 0x80000000u;
constexpr int NO = 64;
constexpr int SPIN_CAP = 1 << 19;
constexpr u64 TAG_STEP = 0x9E3779B97F4A7C15ull;

constexpr int WS_EPOCH = 0;
constexpr int WS_RANK = 4096;
constexpr int WS_STAT = 8192;
constexpr int WS_QG = 16384;
constexpr int WS_PART = WS_QG + 8 * 16 * 4096;
constexpr int WS_Z = WS_PART + 256 * 1024;
constexpr int WS_H = WS_Z + 32 * 18432;
constexpr int WS_Y = WS_H + 8 * 18432;
constexpr int WS_END = WS_Y + 72 * 18432;

constexpr int XROW = 1168;
constexpr int L_XN = 0;
constexpr int L_QS = L_XN + 8 * XROW;
constexpr int L_ST = L_QS + 512;
constexpr int L_P = L_ST + 240;
constexpr int L_MRG = L_P + 7 * 768;
constexpr int OROW = 272;
constexpr int L_OA = L_MRG + 7168;
constexpr int L_RED = L_OA + 8 * OROW;
constexpr int L_U = L_RED + 8 * 2592;
constexpr int L_GR = L_U + 1152;
constexpr int L_WT = L_GR + 640;
constexpr int L_W2 = L_WT + 576 * 128;
constexpr int L_END = L_W2 + 64 * 576;
static_assert(L_WT % 16 == 0 && L_MRG % 16 == 0 && L_P % 16 == 0 && L_OA % 16 == 0 && L_RED % 16 == 0 && L_QS % 16 == 0, "align");
static_assert(L_END > 81920 && L_END <= 163840, "one workgroup per CU");

typedef __amdgpu_buffer_rsrc_t rsrc_t;
__device__ __forceinline__ rsrc_t mk_rsrc(const void* p, unsigned bytes) {
  u64 a = (u64)p;
  unsigned lo = __builtin_amdgcn_readfirstlane((unsigned)a), hi = __builtin_amdgcn_readfirstlane((unsigned)(a >> 32));
  return __builtin_amdgcn_make_buffer_rsrc((void*)(((u64)hi << 32) | lo), 0, __builtin_amdgcn_readfirstlane(bytes), 0x00020000);
}
template <int AUX> __device__ __forceinline__ v4u ld128(rsrc_t r, unsigned vo, unsigned so = 0) { return __builtin_amdgcn_raw_buffer_load_b128(r, vo, so, AUX); }
template <int AUX> __device__ __forceinline__ unsigned ld32(rsrc_t r, unsigned vo, unsigned so = 0) { return __builtin_amdgcn_raw_buffer_load_b32(r, vo, so, AUX); }
__device__ __forceinline__ void dma128(rsrc_t r, LAS unsigned char* dst, unsigned vo, unsigned so) {
  const unsigned d = __builtin_amdgcn_readfirstlane((unsigned)(size_t)dst), sof = __builtin_amdgcn_readfirstlane(so);
  unsigned keep;
  asm volatile("s_mov_b32 %0, m0\n\ts_mov_b32 m0, %1\n\ts_nop 3\n\tbuffer_load_dwordx4 %2, %3, %4 offen nt lds\n\ts_mov_b32 m0, %0" : "=&s"(keep) : "s"(d), "v"(vo), "s"(r), "s"(sof) : "memory");
}
template <int AUX = AUX_SC1> __device__ __forceinline__ void st128(v4u v, rsrc_t r, unsigned vo) { __builtin_amdgcn_raw_buffer_store_b128(v, r, vo, 0, AUX); }
__device__ __forceinline__ void st64(v2u v, rsrc_t r, unsigned vo) { __builtin_amdgcn_raw_buffer_store_b64(v, r, vo, 0, AUX_SC1); }

__device__ __forceinline__ float u2f(unsigned u) { return __builtin_bit_cast(float, u); }
__device__ __forceinline__ unsigned f2u(float f) { return __builtin_bit_cast(unsigned, f); }
__device__ __forceinline__ f32x4 u2f4(v4u u) { return __builtin_bit_cast(f32x4, u); }
__device__ __forceinline__ unsigned pk2(float lo, float hi) { f32x2 v = {lo, hi}; return __builtin_bit_cast(unsigned, __builtin_convertvector(v, bf16x2)); }
__device__ __forceinline__ float blo(unsigned u) { return u2f(u << 16); }
__device__ __forceinline__ float bhi(unsigned u) { return u2f(u & 0xFFFF0000u); }
__device__ __forceinline__ float rb(float f) { return blo(pk2(f, f)); }
__device__ __forceinline__ v4u tag_bytes(unsigned tag) { v4u t = {tag & 255u, (tag >> 8) & 255u, (tag >> 16) & 255u, tag >> 24}; asm volatile("" : "+v"(t)); return t; }
__device__ __forceinline__ v4u emb4(f32x4 v, v4u tb) { return (__builtin_bit_cast(v4u, v) & 0xFFFFFF00u) | tb; }
__device__ __forceinline__ unsigned or4(v4u x) { return x.x | x.y | x.z | x.w; }
__device__ __forceinline__ f32x4 sel4(bool c, f32x4 a, f32x4 b) { asm volatile("" : "+v"(a), "+v"(b)); return c ? a : b; }
__device__ __forceinline__ unsigned chkg(v4u x, unsigned tag) { return (x.y ^ tag) | (x.w ^ tag); }
__device__ __forceinline__ bool none(unsigned bad) { asm volatile("" : "+v"(bad)); return __builtin_amdgcn_ballot_w64(bad != 0u) == 0ull; }

template <int CTRL> __device__ __forceinline__ float dpp(float x) { return u2f(__builtin_amdgcn_mov_dpp(f2u(x), CTRL, 0xf, 0xf, true)); }
constexpr int XOR1 = 0xB1, XOR2 = 0x4E, XOR7 = 0x141, XOR8 = 0x128;
__device__ __forceinline__ float x16_sum(float x) { auto s = __builtin_amdgcn_permlane16_swap(f2u(x), f2u(x), false, false); return u2f(s[0]) + u2f(s[1]); }
__device__ __forceinline__ float x32_sum(float x) { auto t = __builtin_amdgcn_permlane32_swap(f2u(x), f2u(x), false, false); return u2f(t[0]) + u2f(t[1]); }
__device__ __forceinline__ float tsum4(f32x4 v) {
  auto a = __builtin_amdgcn_permlane32_swap(f2u(v.x), f2u(v.z), false, false), b = __builtin_amdgcn_permlane32_swap(f2u(v.y), f2u(v.w), false, false);
  auto c = __builtin_amdgcn_permlane16_swap(f2u(u2f(a[0]) + u2f(a[1])), f2u(u2f(b[0]) + u2f(b[1])), false, false);
  const float t = u2f(c[0]) + u2f(c[1]);
  return t + dpp<XOR8>(t);
}
__device__ __forceinline__ float row_sum(float x) { x += dpp<XOR1>(x); x += dpp<XOR2>(x); x += dpp<XOR7>(x); x += dpp<XOR8>(x); return x; }
__device__ __forceinline__ f32x4 mfma(v4u a, v4u b, f32x4 c) { return __builtin_amdgcn_mfma_f32_16x16x32_bf16(__builtin_bit_cast(bf16x8, a), __builtin_bit_cast(bf16x8, b), c, 0, 0, 0); }
__device__ __forceinline__ void bar() { __syncthreads(); }
__device__ __forceinline__ v4u trfrag(LAS unsigned char* L, int a, int rowbytes) {
  v2u lo = __builtin_bit_cast(v2u, __builtin_amdgcn_ds_read_tr16_b64_v4i16((LAS s16x4*)(L + a)));
  v2u hi = __builtin_bit_cast(v2u, __builtin_amdgcn_ds_read_tr16_b64_v4i16((LAS s16x4*)(L + a + 4 * rowbytes)));
  v4u r = {lo.x, lo.y, hi.x, hi.y}; return r;
}
__device__ __forceinline__ int traddr(int lane, int rowbytes) { return (8 * (lane >> 4) + ((lane & 15) >> 2)) * rowbytes + (lane & 3) * 8; }
__device__ __forceinline__ float ln_inv(float var) { return __builtin_amdgcn_rcpf(rb(__builtin_amdgcn_sqrtf(rb(rb(var) + 1e-5f)))); }
__device__ __forceinline__ unsigned lds_count(LAS unsigned char* L, int off) { return __builtin_amdgcn_readfirstlane(*(volatile LAS unsigned*)(L + off)); }
__device__ __forceinline__ void lds_inc(LAS unsigned char* L, int off, int lane) {
  asm volatile("s_waitcnt lgkmcnt(0)" ::: "memory");
  if (lane == 0) asm volatile("ds_add_u32 %0, %1" :: "v"((unsigned)(size_t)(L + off)), "v"(1u) : "memory");
}
template <bool NAP = true> __device__ __forceinline__ void lds_wait(LAS unsigned char* L, int off, unsigned want) {
#pragma nounroll
  for (int spin = 0; spin < SPIN_CAP * 16 && lds_count(L, off) < want; ++spin) if (NAP) __builtin_amdgcn_s_sleep(1);
  asm volatile("" ::: "memory");
}

template <int I> struct IC { static constexpr int v = I; };
template <int A, int B, class F> __device__ __forceinline__ void static_for(F&& fn) { if constexpr (A < B) { fn(IC<A>{}); static_for<A + 1, B>(fn); } }
template <int N> __device__ __forceinline__ void wait_vm() { asm volatile("s_waitcnt vmcnt(%0)" :: "n"(N) : "memory"); }
constexpr int cmin(int a, int b) { return a < b ? a : b; }
constexpr int cmax(int a, int b) { return a > b ? a : b; }
constexpr int NK = 2 * ((TOTB + 13) / 14), NU = (NK + 7) / 8;
constexpr int cq(int n) { return n < QS ? 0 : cmin(cmin(2 * (n - QS + 1), cmax(0, n - PACE + 1) / 2), NK / 2); }
constexpr int nsm() { int n = 0; while (cq(n) < NK / 2) ++n; return n; }
constexpr int cp(int n) { return n <= nsm() ? 0 : cmin(2 * (n - nsm()), cmax(0, n - PACE + 1 - NK) / 2); }
constexpr int vwin() { int m = 0; for (int n = NK; n < 2 * NK; ++n) m = cmax(m, n - NK + 1 - 2 * cp(n - 1)); return m; }
constexpr int VW = vwin();
constexpr int PADMAX = NK - (512 - TOTB) / 7;
static_assert(NU <= 6 && TOTB >= 256 && nsm() < 2 * NK - 1, "schedule");

#define SB() __builtin_amdgcn_sched_barrier(0)
#define REPOLL(off) asm volatile("" : "+v"(off) :: "memory")
#define PIN(x) asm volatile("" : "+v"(x))

__global__ void __launch_bounds__(512, 2) fused_decode_block(const u16* __restrict__ x, const u16* __restrict__ kc, const u16* __restrict__ vc, const u16* __restrict__ wq,
    unsigned char* ws, const u16* __restrict__ wo, const u16* __restrict__ w1, const u16* __restrict__ w2, u16* __restrict__ out) {
  __shared__ __attribute__((aligned(16))) unsigned char smem[L_END];
  LAS unsigned char* L = (LAS unsigned char*)smem;
  const int tid = threadIdx.x, lane = tid & 63, w = __builtin_amdgcn_readfirstlane(tid >> 6);
  constexpr int C_Q = L_ST + 192, C_K = L_ST + 200, C_F = L_ST + 204, C_M = L_ST + 208, C_V = L_ST + 212, C_T = L_ST + 216, C_R = L_ST + 220;
  if (tid < 12) *(LAS unsigned*)(L + C_Q + tid * 4) = 0u;
  const int blk = blockIdx.x, s = blk & 1, xg = blk & 7, j = blk >> 3;
  const int kvh = 4 * (xg >> 1) + (j >> 3), b = j & 7;
  const int r4 = lane >> 4, c16 = lane & 15;
  const int sw = w - 1;
  const int tot = s ? 512 - TOTB : TOTB, base = tot / 7, rem = tot - 7 * base;
  int nk = base + (sw < rem ? 1 : 0);
  size_t kvoff = ((size_t)(b * HKV + kvh) * SK + (SK - WIN) + (s ? 4 * TOTB : 0) + 4 * (sw * base + min(sw, rem))) * 128;
  asm volatile("" : "+s"(kvoff), "+s"(nk));
  asm volatile("" :: "s"(x), "s"(kc), "s"(vc), "s"(wq), "s"(ws));
  bar();
  const rsrc_t rws = mk_rsrc(ws, WS_END);
  const rsrc_t rx = mk_rsrc(x, 8 * D * 2);
  LAS float* stm = (LAS float*)(L + L_ST);
  const __attribute__((address_space(4))) u64* epoch_p = (const __attribute__((address_space(4))) u64*)(ws + WS_EPOCH);
  u64 tag64;

  if (w == 0) {
    __builtin_amdgcn_s_setprio(3);
    const int kq = b >> 1, qcol = s * 128 + (b & 1) * 64, row = lane >> 3, part = lane & 7;
    v4u xa[9], xs[3];
#pragma unroll
    for (int i = 0; i < 9; ++i) xa[i] = ld128<0>(rx, row * 1152 + part * 16 + i * 128);
#pragma unroll
    for (int i = 0; i < 3; ++i) xs[i] = ld128<0>(rx, row * 1152 + kq * 288 + part * 16 + i * 128);
    const rsrc_t rq = mk_rsrc(wq + (144 * kq) * NQ + kvh * 256 + qcol, 143 * NQ * 2 + 128);
#pragma unroll
    for (int t = 0; t < 18; ++t) __builtin_amdgcn_raw_ptr_buffer_load_lds(rq, (LAS void*)(L + L_W2 + t * 1024), 16, row * (NQ * 2) + part * 16, t * 8 * NQ * 2, 0, AUX_NT);
    tag64 = *epoch_p;
    SB();
    {
      f32x2 e[36];
#pragma unroll
      for (int i = 0; i < 36; ++i) e[i] = f32x2{blo(xa[i >> 2][i & 3]), bhi(xa[i >> 2][i & 3])};
      f32x2 s4[4] = {e[0], e[1], e[2], e[3]};
#pragma unroll
      for (int i = 4; i < 36; ++i) s4[i & 3] += e[i];
      const f32x2 s2 = (s4[0] + s4[1]) + (s4[2] + s4[3]);
      float sum = s2.x + s2.y;
      sum += dpp<XOR1>(sum); sum += dpp<XOR2>(sum); sum += dpp<XOR7>(sum);
      const float mean = sum * (1.0f / D);
      f32x2 v4[4] = {{0.f, 0.f}, {0.f, 0.f}, {0.f, 0.f}, {0.f, 0.f}};
#pragma unroll
      for (int i = 0; i < 36; ++i) { const f32x2 d = e[i] - mean; v4[i & 3] += d * d; }
      const f32x2 v2 = (v4[0] + v4[1]) + (v4[2] + v4[3]);
      float var = v2.x + v2.y;
      var += dpp<XOR1>(var); var += dpp<XOR2>(var); var += dpp<XOR7>(var);
      const float inv = ln_inv(var * (1.0f / D)), m = rb(mean);
#pragma unroll
      for (int i = 0; i < 3; ++i) {
        v4u o;
#pragma unroll
        for (int d = 0; d < 4; ++d) { const unsigned dd = pk2(blo(xs[i][d]) - m, bhi(xs[i][d]) - m); o[d] = pk2(blo(dd) * inv, bhi(dd) * inv); }
        *(LAS v4u*)(L + L_XN + row * XROW + part * 16 + i * 128) = o;
      }
    }
    asm volatile("s_waitcnt vmcnt(0) lgkmcnt(0)" ::: "memory");
    tag64 += TAG_STEP;
    asm volatile("" : "+s"(tag64));
    const unsigned tag = (unsigned)tag64;
    const v4u tn = {tag & 15u, (tag >> 4) & 15u, (tag >> 8) & 15u, (tag >> 12) & 15u};
    {
      const int ta = L_W2 + traddr(lane, 128), xb = L_XN + (lane & 7) * XROW;
      v4u fa[4];
#pragma unroll
      for (int ks = 0; ks < 4; ++ks) fa[ks] = *(LAS v4u*)(L + xb + r4 * 16 + ks * 64);
      const s16x4 ga = *(LAS s16x4*)(L + xb + 256 + r4 * 8);
      v4u fb[4][4]; s16x4 gb[4];
#pragma unroll
      for (int t = 0; t < 4; ++t) {
#pragma unroll
        for (int ks = 0; ks < 4; ++ks) fb[t][ks] = trfrag(L, ta + t * 32 + ks * 4096, 128);
        gb[t] = __builtin_amdgcn_ds_read_tr16_b64_v4i16((LAS s16x4*)(L + L_W2 + (128 + (lane >> 2)) * 128 + (lane & 3) * 8 + t * 32));
      }
      SB();
      f32x4 qt[4];
#pragma unroll
      for (int t = 0; t < 4; ++t) {
        const f32x4 qb = __builtin_amdgcn_mfma_f32_16x16x16bf16_1k(gb[t], ga, f32x4{0.f, 0.f, 0.f, 0.f}, 0, 0, 0);
        f32x4 qa = {0.f, 0.f, 0.f, 0.f};
#pragma unroll
        for (int ks = 0; ks < 4; ++ks) qa = mfma(fb[t][ks], fa[ks], qa);
        qt[t] = qa + qb;
      }
#pragma unroll
      for (int t = 0; t < 2; ++t) *(LAS f32x4*)(L + L_OA + (lane & 7) * OROW + (2 * t + (c16 >> 3)) * 64 + r4 * 16) = sel4(c16 < 8, qt[2 * t], qt[2 * t + 1]);
      asm volatile("s_waitcnt lgkmcnt(0)" ::: "memory");
#pragma unroll
      for (int i = 0; i < 2; ++i)
        __builtin_amdgcn_raw_buffer_store_b128(((*(LAS v4u*)(L + L_OA + (r4 + 4 * i) * OROW + c16 * 16) + 8u) & 0xFFFFFFF0u) | tn, rws,
                                               WS_QG + (((r4 + 4 * i) * 16 + kvh) * 4 + kq) * 1024 + qcol * 4 + c16 * 16, 0, AUX_SC1 | AUX_NT);
    }
    __builtin_amdgcn_s_sleep(QDELAY);
    {
      v4u qg[4];
      int qo = WS_QG + (b * 16 + kvh) * 4096 + lane * 16;
#pragma nounroll
      for (int spin = 0; spin < SPIN_CAP; ++spin) {
        REPOLL(qo);
#pragma unroll
        for (int i = 0; i < 4; ++i) qg[i] = ld128<AUX_SC1 | AUX_NT>(rws, qo + i * 1024) ^ tn;
        if (none(or4(qg[0] | qg[1] | qg[2] | qg[3]) & 15u)) break;
      }
      const f32x4 q4 = (u2f4(qg[0]) + u2f4(qg[1])) + (u2f4(qg[2]) + u2f4(qg[3]));
      *(LAS v2u*)(L + L_QS + lane * 8) = v2u{pk2(q4.x, q4.y), pk2(q4.z, q4.w)};
      lds_inc(L, C_Q, lane);
    }
    if (j < 8) {
      lds_wait(L, C_T, 7);
      st128(v4u{(unsigned)__builtin_amdgcn_s_memrealtime(), tag, 0u, (unsigned)(tag64 >> 32)}, rws, lane == 0 ? WS_RANK + (xg * 8 + j) * 16 : OOB);
    }
    lds_wait(L, C_K, 7);
    {
      const int hh = (lane >> 3) & 1, ww = lane & 7;
      const float mw = ww < 7 ? stm[hh * 8 + ww] : -INFINITY, lw = ww < 7 ? stm[16 + hh * 8 + ww] : 0.f;
      float M = fmaxf(mw, dpp<XOR1>(mw)); M = fmaxf(M, dpp<XOR2>(M)); M = fmaxf(M, dpp<XOR7>(M));
      const float fac = __expf(mw - M);
      float lt = fac * lw; lt += dpp<XOR1>(lt); lt += dpp<XOR2>(lt); lt += dpp<XOR7>(lt);
      if (lane < 16) stm[32 + lane] = fac;
      lds_inc(L, C_F, lane);
      st128(v4u{f2u(M), tag, f2u(lt), tag}, rws, (lane & 55) == 0 ? WS_STAT + ((b * 16 + kvh) * 2 + s) * 32 + hh * 16 : OOB);
    }
    {
      __builtin_amdgcn_s_sleep(RDELAY);
      int ro = WS_RANK + lane * 16;
      v4u rv;
#pragma nounroll
      for (int spin = 0; spin < SPIN_CAP; ++spin) {
        REPOLL(ro);
        rv = ld128<AUX_SC1>(rws, ro);
        if (none((rv.y ^ tag) | (rv.w ^ (unsigned)(tag64 >> 32)))) break;
        __builtin_amdgcn_s_sleep(16);
      }
      unsigned tu = rv.x - __builtin_amdgcn_readfirstlane(rv.x);
      tu += f2u(dpp<XOR1>(u2f(tu))); tu += f2u(dpp<XOR2>(u2f(tu))); tu += f2u(dpp<XOR7>(u2f(tu)));
      const int t = (int)tu;
      const int mine = __builtin_amdgcn_readlane(t, xg * 8);
      const u64 ahead = __builtin_amdgcn_ballot_w64((lane & 7) == 0 && (t < mine || (t == mine && (lane >> 3) < xg)));
      const int rl = __builtin_popcountll(ahead) * 32 + j;
      if (lane == 0) *(volatile LAS unsigned*)(L + C_R) = rl + 1;
    }
    lds_wait(L, C_V, 7);
  } else {
    const rsrc_t rk = mk_rsrc(kc + kvoff, nk * 1024), rv = mk_rsrc(vc + kvoff, nk * 1024);
    v4u kr[NK], vr[VW], bf[8], qa1, qa2, pa;
    f32x4 acc[8];
    float sc[NU], dot = 0.f;
    const int klo = ((lane >> 2) & 3) * 256 + r4 * 64 + (lane & 3) * 16;
    const int jj_l = 2 * (lane & 3) + (lane >> 5);

    auto qk_pair = [&](auto P_) {
      constexpr int g0 = NK - 2 - 2 * decltype(P_)::v, u = g0 / 8, pr = (g0 % 8) / 2;
      const f32x4 dd = mfma(qa2, kr[g0 + 1], mfma(qa1, kr[g0], f32x4{0.f, 0.f, 0.f, 0.f}));
      float t = dpp<0x00>(dd.x); t += dpp<0x55>(dd.y); t += dpp<0xAA>(dd.z); t += dpp<0xFF>(dd.w);
      dot = (lane & 3) == pr ? t : dot;
      if constexpr (pr == 0) {
        float sv = rb(rb(dot) * 0.08838834764831845f);
        sv = (u * 8 + jj_l < nk) ? sv : -INFINITY;
        PIN(sv);
        sc[u] = sv;
      } else PIN(dot);
    };
    auto pv_pair = [&](auto P_) {
      constexpr int p = decltype(P_)::v, i0 = NK - 2 - 2 * p, u = i0 / 8, t = (i0 % 8) / 2;
      if constexpr (t == 3 || p == 0) pa = *(LAS v4u*)(L + L_P + sw * 768 + ((u * 4 + r4) * 2 + ((c16 >> 2) & 1)) * 16);
      const v4u c = vr[(2 * p) % VW], a = vr[(2 * p + 1) % VW];
#pragma unroll
      for (int d = 0; d < 4; ++d) { bf[2 * d][t] = __builtin_amdgcn_perm(c[d], a[d], 0x05040100u); bf[2 * d + 1][t] = __builtin_amdgcn_perm(c[d], a[d], 0x07060302u); }
      if constexpr (p == 0) {
#pragma unroll
        for (int c2 = 0; c2 < 8; ++c2)
#pragma unroll
          for (int t2 = t + 1; t2 < 4; ++t2) bf[c2][t2] = 0u;
      }
      if constexpr (t == 0) {
#pragma unroll
        for (int c2 = 0; c2 < 8; ++c2) acc[c2] = mfma(pa, bf[c2], u == NU - 1 ? f32x4{0.f, 0.f, 0.f, 0.f} : acc[c2]);
      } else {
#pragma unroll
        for (int d = 0; d < 8; ++d) PIN(bf[d][t]);
      }
      SB();
    };

    static_for<0, 2 * NK>([&](auto N_) {
      constexpr int n = decltype(N_)::v;
      if constexpr (n >= NK && n < NK + PADMAX + PACE) wait_vm<PACE - 1 + cmin(n - NK + 1, PADMAX)>(); else if constexpr (n >= PACE) wait_vm<PACE - 1>();
      if constexpr (n < NK) kr[NK - 1 - n] = ld128<AUX_NT>(rk, klo, (NK - 1 - n) * 1024);
      else vr[(n - NK) % VW] = ld128<AUX_NT>(rv, lane * 16, (2 * NK - 1 - n) * 1024);
      SB();
      if constexpr (n == MSTEP) lds_inc(L, C_T, lane);
      if constexpr (n == 2 * NK - 1) lds_inc(L, C_V, lane);
      if constexpr (n == QS - 4) { tag64 = *epoch_p; SB(); }
      if constexpr (n == QS) {
        tag64 += TAG_STEP;
        asm volatile("" : "+s"(tag64));
        lds_wait(L, C_Q, 1);
        const v4u qv = *(LAS v4u*)(L + L_QS + ((c16 >> 2) & 1) * 256 + (4 * r4 + (lane & 3)) * 16), z4 = {0u, 0u, 0u, 0u};
        qa1 = c16 < 8 ? qv : z4; qa2 = c16 < 8 ? z4 : qv;
      }
      static_for<cq(n - 1), cq(n)>(qk_pair);
      if constexpr (n == nsm()) {
        float mx = sc[0];
#pragma unroll
        for (int u = 1; u < NU; ++u) mx = fmaxf(mx, sc[u]);
        mx = fmaxf(mx, dpp<XOR1>(mx)); mx = fmaxf(mx, dpp<XOR2>(mx)); mx = fmaxf(mx, dpp<XOR7>(mx)); mx = fmaxf(mx, dpp<XOR8>(mx));
        { auto t = __builtin_amdgcn_permlane32_swap(f2u(mx), f2u(mx), false, false); mx = fmaxf(u2f(t[0]), u2f(t[1])); }
        float ls = 0.f;
#pragma unroll
        for (int u = 0; u < NU; ++u) {
          const float p = __expf(sc[u] - mx);
          ls += p;
          *(LAS u16*)(L + L_P + sw * 768 + ((u * 4 + ((lane >> 2) & 3)) * 2 + (r4 & 1)) * 16 + jj_l * 2) = (u16)pk2(p, p);
        }
        ls = x32_sum(row_sum(ls));
        if ((lane & 47) == 0) { stm[r4 * 8 + sw] = mx; stm[16 + r4 * 8 + sw] = ls; }
        lds_inc(L, C_K, lane);
      }
      static_for<cp(n - 1), cp(n)>(pv_pair);
      SB();
    });
    lds_wait<false>(L, C_F, 1);
    f32x4 fa = *(LAS f32x4*)(stm + 32 + (lane >> 5) * 8), fb = *(LAS f32x4*)(stm + 36 + (lane >> 5) * 8);
    int po = WS_PART + ((b * 16 + kvh) * 2 + s) * 1024 + lane * 16, mw = L_MRG + sw * 1024 + (r4 & 1) * 512 + c16 * 32 + (r4 >> 1) * 16;
    const v4u tby = tag_bytes((unsigned)tag64);
    PIN(fa); PIN(fb); PIN(po); PIN(mw);
    static_for<cp(2 * NK - 1), NK / 2>(pv_pair);
    {
      const bool hi = lane >= 32;
      f32x4 o4 = {hi ? acc[4].x : acc[0].x, hi ? acc[5].x : acc[1].x, hi ? acc[6].x : acc[2].x, hi ? acc[7].x : acc[3].x};
      *(LAS f32x4*)(L + mw) = o4;
    }
    unsigned ticket = 1u;
    if (lane == 0) asm volatile("ds_add_rtn_u32 %0, %1, %0" : "+v"(ticket) : "v"((unsigned)(size_t)(L + C_M)) : "memory");
    f32x4 t[7];
#pragma unroll
    for (int ww = 0; ww < 7; ++ww) t[ww] = *(LAS f32x4*)(L + L_MRG + ww * 1024 + lane * 16);
    asm volatile("s_waitcnt lgkmcnt(0)" : "+v"(ticket) :: "memory");
    if (__builtin_amdgcn_readfirstlane(ticket) == 6) {
      const f32x4 o4 = ((t[0] * fa.x + t[1] * fa.y) + (t[2] * fa.z + t[3] * fa.w)) + ((t[4] * fb.x + t[5] * fb.y) + t[6] * fb.z);
      st128(emb4(o4, tby), rws, po);
      lds_inc(L, C_M, lane);
    }
  }
  const unsigned tag = __builtin_amdgcn_readfirstlane((unsigned)tag64), tagh = __builtin_amdgcn_readfirstlane((unsigned)(tag64 >> 32));

  {
  int tid = threadIdx.x;
  asm volatile("" : "+v"(tid));
  const int lane = tid & 63, r4 = lane >> 4, c16 = lane & 15;
  lds_wait(L, C_R, 1);
  const int role = (lds_count(L, C_R) - 1) & 255;
  const v4u tby = tag_bytes(tag);
  rsrc_t rout = mk_rsrc(out, 8 * D * 2);
  asm volatile("" : "+s"(rout));
  const int job = role >= NO && role < NO + 144 ? role - NO : -1;
  int yo = WS_Y + (9 * w + (lane >> 3)) * 18432 + job * 128 + (lane & 7) * 16, yo2 = WS_Y + (9 * w + 8) * 18432 + job * 128 + (lane & 7) * 16;
  int fw = L_OA + (r4 * 8 + (lane & 7)) * 48 + w * 4, fr = L_OA + (lane & 31) * 48 + (lane >> 5) * 16;
  unsigned oo = lane < 32 ? (lane & 7) * 1152 + job * 8 + (lane >> 3) * 2 : OOB;
  float hres;
  u64 ydue = 0;
  if (role < NO) {
    const int ohq = role >> 1, ocg = role & 1;
    {
      const rsrc_t ro = mk_rsrc(wo + (128 * ohq + 16 * w) * D + 288 * ocg, 15 * D * 2 + 576);
#pragma unroll
      for (int k = 0; k < 9; ++k) { const int ch = 64 * k + lane, rr = ch / 36, pc = ch - rr * 36; dma128(ro, L + L_WT + w * 9216 + k * 1024, rr * (D * 2) + ((pc ^ ((rr >> 2) & 2)) * 16), 0); }
    }
    if (w == 0) {
      int so = WS_STAT + (((lane >> 1) & 7) * 16 + (ohq >> 1)) * 64 + (lane & 1) * 32 + (ohq & 1) * 16;
      v4u sa;
#pragma nounroll
      for (int spin = 0; spin < SPIN_CAP; ++spin) {
        REPOLL(so);
        sa = ld128<AUX_SC1>(rws, so);
        if (none(chkg(sa, tag))) break;
      }
      const float M = fmaxf(u2f(sa.x), dpp<XOR1>(u2f(sa.x))), e = __expf(u2f(sa.x) - M), el = e * u2f(sa.z), wgt = e / (el + dpp<XOR1>(el));
      if (lane < 16) *(LAS float*)(L + L_RED + lane * 4) = wgt;
    }
    const int ta = L_WT + traddr(lane, 576) + ((2 * w) ^ (r4 & 1)) * 32, tb = L_WT + traddr(lane, 576) + ((2 * w + 1) ^ (r4 & 1)) * 32, tc = L_WT + traddr(lane, 576) + ((16 + (w & 1)) ^ (r4 & 1)) * 32;
    int ow = L_XN + w * OROW + (lane & 31) * 8 + (lane >> 5) * 4, oa = L_XN + (lane & 7) * OROW + r4 * 16;
    int zo = WS_Z + ohq * 18432 + (ocg * 18 + 2 * w + (c16 >> 3)) * 512 + r4 * 128 + (lane & 7) * 16;
    unsigned zo2 = c16 < 8 ? WS_Z + ohq * 18432 + (ocg * 18 + 16 + (w & 1)) * 512 + r4 * 128 + c16 * 16 : OOB;
    int po = WS_PART + (w * 16 + (ohq >> 1)) * 2048 + (lane >> 5) * 1024 + (ohq & 1) * 512 + (lane & 31) * 16;
    PIN(ow); PIN(oa); PIN(zo); PIN(zo2); PIN(po);
    wait_vm<0>();
    bar();
    v4u fa[4], fb[4], fc[4], pa = ld128<AUX_SC1>(rws, po);
    SB();
#pragma unroll
    for (int ks = 0; ks < 4; ++ks) { fa[ks] = trfrag(L, ta + ks * 32 * 576, 576); fb[ks] = trfrag(L, tb + ks * 32 * 576, 576); fc[ks] = trfrag(L, tc + ks * 32 * 576, 576); }
    float ee = *(LAS float*)(L + L_RED + (w * 2 + (lane >> 5)) * 4);
    PIN(ee);
#pragma unroll
    for (int ks = 0; ks < 4; ++ks) { PIN(fa[ks]); PIN(fb[ks]); PIN(fc[ks]); }
#pragma nounroll
    for (int spin = 0; spin < SPIN_CAP; ++spin) {
      pa ^= tby;
      if (none(or4(pa) & 255u)) break;
      REPOLL(po);
      pa = ld128<AUX_SC1>(rws, po);
    }
    {
      const f32x4 o4 = u2f4(pa) * ee;
      auto s0 = __builtin_amdgcn_permlane32_swap(f2u(o4.x), f2u(o4.z), false, false), s1 = __builtin_amdgcn_permlane32_swap(f2u(o4.y), f2u(o4.w), false, false);
      *(LAS unsigned*)(L + ow) = pk2(u2f(s0[0]) + u2f(s0[1]), u2f(s1[0]) + u2f(s1[1]));
    }
    bar();
    {
      v4u ob[4];
#pragma unroll
      for (int ks = 0; ks < 4; ++ks) ob[ks] = *(LAS v4u*)(L + oa + ks * 64);
      SB();
      f32x4 za = {0.f, 0.f, 0.f, 0.f}, zb = {0.f, 0.f, 0.f, 0.f}, zc = {0.f, 0.f, 0.f, 0.f};
#pragma unroll
      for (int ks = 0; ks < 4; ++ks) { za = mfma(fa[ks], ob[ks], za); zb = mfma(fb[ks], ob[ks], zb); }
      st128(emb4(c16 < 8 ? za : zb, tby), rws, zo);
      if (w < 2) {
#pragma unroll
        for (int ks = 0; ks < 4; ++ks) zc = mfma(fc[ks], ob[ks], zc);
        st128(emb4(zc, tby), rws, zo2);
      }
    }
  } else if (role >= NO + 144) {
    if (w < 3) {
      const int cg = 3 * (role - NO - 144) + w;
      float xf = blo(__builtin_amdgcn_raw_buffer_load_b16(rx, (lane & 7) * 1152 + cg * 8 + r4 * 2, 0, 0));
      int rzo = WS_Z + (lane >> 3) * 18432 + cg * 128 + (lane & 7) * 16, ho = WS_H + (lane >> 3) * 18432 + cg * 128 + (lane & 7) * 16;
      PIN(ho);
      lds_wait(L, C_M, 8);
      v4u z[4];
#pragma nounroll
      for (int spin = 0; spin < SPIN_CAP; ++spin) {
        REPOLL(rzo);
#pragma unroll
        for (int i = 0; i < 4; ++i) z[i] = ld128<AUX_SC1>(rws, rzo, i * (8 * 18432)) ^ tby;
        if (none(or4(z[0] | z[1] | z[2] | z[3]) & 255u)) break;
      }
      const float hv = xf + rb(tsum4((u2f4(z[0]) + u2f4(z[1])) + (u2f4(z[2]) + u2f4(z[3]))));
      auto p = __builtin_amdgcn_permlane16_swap(f2u(hv), f2u(hv), false, false);
      const unsigned pr = pk2(u2f(p[0]), u2f(p[1]));
      auto q = __builtin_amdgcn_permlane32_swap(pr, pr, false, false);
      st128(v4u{q[0], tag, q[1], tag}, rws, ho);
    }
  } else {
    const int hg = job >> 1, ch = job & 1;
    unsigned prh = ld32<AUX_SC1>(rws, lane < 18 ? WS_H + xg * 18432 + (18 * w + lane) * 128 : OOB);
    SB();
    {
      const rsrc_t r1 = mk_rsrc(w1 + 64 * hg + w * 72 * DFF, 71 * DFF * 2 + 128), r2 = mk_rsrc(w2 + (64 * hg + 8 * w) * D + 288 * ch, 7 * D * 2 + 576);
      const unsigned v1 = (lane >> 3) * (DFF * 2) + ((lane & 7) ^ ((lane >> 3) & 2) ^ ((w & 1) << 2)) * 16;
#pragma unroll
      for (int k = 0; k < 9; ++k) {
        if (k == KPUB) lds_wait<false>(L, C_M, 8);
        dma128(r1, L + L_WT + w * 9216 + k * 1024, v1 ^ ((k & 1) << 6), k * (8 * DFF * 2));
      }
#pragma unroll
      for (int k = 0; k < 5; ++k) { const int c = 64 * k + lane, rr = c / 36, pc = c - rr * 36;
        if (k < 4 || lane < 32) dma128(r2, L + L_W2 + w * 4608 + k * 1024, rr * (D * 2) + ((pc ^ ((w & 1) << 1)) * 16), 0); }
    }
    int hw = L_XN + (lane & 7) * XROW + (tid >> 3) * 8;
    int rw_ = L_RED + w * 2592 + c16 * 288 + r4 * 16, gw = L_GR + (lane & 7) * 80 + (lane >> 5) * 32 + w * 4;
    int uw = L_U + (lane & 7) * 144 + ((tid >> 3) & 31) * 4, ur = L_U + (lane & 7) * 144 + r4 * 16;
    PIN(uw); PIN(ur);
    int co = L_RED + 8 * 288 + ((tid >> 3) & 31) * 8, ro = L_RED + (lane & 7) * 288 + ((tid >> 3) & 31) * 8, gs = L_GR + (lane & 7) * 80;
    int yw = WS_Y + hg * 18432 + (ch * 18 + 2 * w + (c16 >> 3)) * 512 + r4 * 128 + (lane & 7) * 16;
    unsigned yw2 = c16 < 8 ? WS_Y + hg * 18432 + (ch * 18 + 16 + (w & 1)) * 512 + r4 * 128 + c16 * 16 : OOB;
    int ho = WS_H + xg * 18432 + tid * 16;
    unsigned ho2 = w < 2 ? ho + 16384 : OOB;
    const int g = ((lane >> 3) & 1) | ((lane >> 3) & 2);
    int ta = L_WT + traddr(lane, 128) + w * (32 * 128);
    int tb = L_W2 + traddr(lane, 576) + ((2 * w) ^ (r4 & 1)) * 32, tc = L_W2 + traddr(lane, 576) + ((2 * w + 1) ^ (r4 & 1)) * 32, td = L_W2 + traddr(lane, 576) + ((16 + (w & 1)) ^ (r4 & 1)) * 32;
    int hr = L_XN + (lane & 7) * XROW + job * 8 + ((lane >> 3) & 3) * 2;
    PIN(hw); PIN(rw_); PIN(gw); PIN(co); PIN(ro); PIN(gs); PIN(yw); PIN(yw2); PIN(ho); PIN(ho2); PIN(ta); PIN(tb); PIN(tc); PIN(td); PIN(hr);
    PIN(yo); PIN(yo2); PIN(fw); PIN(fr); PIN(oo);
    v4u f1[3][4], f2[6], ha, hb_, he;
    PIN(prh);
    wait_vm<0>();
    bar();
    ha = ld128<AUX_SC1>(rws, ho); hb_ = ld128<AUX_SC1>(rws, ho, 8192); he = ld128<AUX_SC1>(rws, ho2);
    SB();
#pragma unroll
    for (int t = 0; t < 4; ++t) {
      const int tt = ta + (t ^ g) * 32;
      f1[0][t] = trfrag(L, tt, 128); f1[1][t] = trfrag(L, tt + 8 * 32 * 128, 128); f1[2][t] = trfrag(L, tt + (w < 2 ? 16 * 32 * 128 : 0), 128);
    }
#pragma unroll
    for (int ks = 0; ks < 2; ++ks) { f2[ks] = trfrag(L, tb + ks * 32 * 576, 576); f2[2 + ks] = trfrag(L, tc + ks * 32 * 576, 576); f2[4 + ks] = trfrag(L, td + ks * 32 * 576, 576); }
#pragma nounroll
    for (int spin = 0; spin < SPIN_CAP; ++spin) {
      if (none(chkg(ha, tag) | chkg(hb_, tag) | (w < 2 ? chkg(he, tag) : 0u))) break;
      REPOLL(ho); REPOLL(ho2);
      ha = ld128<AUX_SC1>(rws, ho); hb_ = ld128<AUX_SC1>(rws, ho, 8192); he = ld128<AUX_SC1>(rws, ho2);
    }
    {
      auto frag = [&](v4u h) { const int one = 0x3F803F80;
        return v4u{c16 < 8 ? h.x : (unsigned)one, c16 < 8 ? h.z : (unsigned)one, (unsigned)__builtin_amdgcn_update_dpp(one, (int)h.x, XOR8, 0xf, 0x3, false), (unsigned)__builtin_amdgcn_update_dpp(one, (int)h.z, XOR8, 0xf, 0x3, false)}; };
      const v4u h0 = frag(ha), h1 = frag(hb_);
      f32x4 ua[4];
#pragma unroll
      for (int t = 0; t < 4; ++t) ua[t] = mfma(f1[0][t], h0, f32x4{0.f, 0.f, 0.f, 0.f});
#pragma unroll
      for (int t = 0; t < 4; ++t) ua[t] = mfma(f1[1][t], h1, ua[t]);
      const f32x2 v0 = {blo(ha.x), bhi(ha.x)}, v1 = {blo(ha.z), bhi(ha.z)}, v2 = {blo(hb_.x), bhi(hb_.x)}, v3 = {blo(hb_.z), bhi(hb_.z)};
      f32x2 s1 = (v0 + v1) + (v2 + v3), s2 = (v0 * v0 + v1 * v1) + (v2 * v2 + v3 * v3);
      *(LAS v2u*)(L + hw) = v2u{ha.x, ha.z};
      *(LAS v2u*)(L + hw + 512) = v2u{hb_.x, hb_.z};
      if (w < 2) {
        const v4u h2 = frag(he);
#pragma unroll
        for (int t = 0; t < 4; ++t) ua[t] = mfma(f1[2][t], h2, ua[t]);
        const f32x2 v4 = {blo(he.x), bhi(he.x)}, v5 = {blo(he.z), bhi(he.z)};
        s1 += v4 + v5; s2 += v4 * v4 + v5 * v5;
        *(LAS v2u*)(L + hw + 1024) = v2u{he.x, he.z};
      }
      auto t = __builtin_amdgcn_permlane32_swap(f2u(s1.x + s1.y), f2u(s2.x + s2.y), false, false);
      float rs = x16_sum(u2f(t[0]) + u2f(t[1]));
      rs += dpp<XOR8>(rs);
      *(LAS float*)(L + gw) = rs;
      if (c16 < 9) {
#pragma unroll
        for (int t2 = 0; t2 < 4; ++t2) *(LAS f32x4*)(L + rw_ + t2 * 64) = ua[t2];
      }
    }
    bar();
    const f32x4 g0 = *(LAS f32x4*)(L + gs), g1 = *(LAS f32x4*)(L + gs + 16);
    f32x2 pa[8], pc[8];
    if (w < 4) {
#pragma unroll
      for (int i = 0; i < 8; ++i) { pa[i] = *(LAS f32x2*)(L + ro + i * 2592); pc[i] = *(LAS f32x2*)(L + co + i * 2592); }
    }
    const f32x4 g2 = *(LAS f32x4*)(L + gs + 32), g3 = *(LAS f32x4*)(L + gs + 48);
    SB();
    const f32x4 gm = g0 + g1;
    const float mean = ((gm.x + gm.z) + (gm.y + gm.w)) * (1.0f / D);
    if (w < 4) {
      const f32x2 asum = ((pa[0] + pa[1]) + (pa[2] + pa[3])) + ((pa[4] + pa[5]) + (pa[6] + pa[7])), csum = ((pc[0] + pc[1]) + (pc[2] + pc[3])) + ((pc[4] + pc[5]) + (pc[6] + pc[7]));
      const f32x2 uv = __builtin_elementwise_maximum(asum - csum * rb(mean), f32x2{0.f, 0.f});
      *(LAS unsigned*)(L + uw) = pk2(uv.x, uv.y);
    }
    SB();
    bar();
    const v4u u0 = *(LAS v4u*)(L + ur), u1 = *(LAS v4u*)(L + ur + 64);
    SB();
    const f32x4 gv = g2 + g3;
    const float inv = ln_inv(fmaxf(fmaf(-mean, mean, ((gv.x + gv.z) + (gv.y + gv.w)) * (1.0f / D)), 0.f));
    {
      const f32x4 ya = mfma(f2[1], u1, mfma(f2[0], u0, f32x4{0.f, 0.f, 0.f, 0.f})), yb = mfma(f2[3], u1, mfma(f2[2], u0, f32x4{0.f, 0.f, 0.f, 0.f}));
      st128(emb4((c16 < 8 ? ya : yb) * inv, tby), rws, yw);
    }
    if (w < 2) st128(emb4(mfma(f2[5], u1, mfma(f2[4], u0, f32x4{0.f, 0.f, 0.f, 0.f})) * inv, tby), rws, yw2);
    ydue = __builtin_amdgcn_s_memrealtime() + YDELAY;
    hres = blo(*(LAS u16*)(L + hr));
    PIN(hres);
  }
  if (job >= 0) {
    while (__builtin_amdgcn_s_memrealtime() < ydue) {}
    v4u y[2];
#pragma nounroll
    for (int spin = 0; spin < SPIN_CAP; ++spin) {
      REPOLL(yo); REPOLL(yo2);
      y[0] = ld128<AUX_SC1>(rws, yo) ^ tby; y[1] = ld128<AUX_SC1>(rws, yo2) ^ tby;
      if (none(or4(y[0] | y[1]) & 255u)) break;
    }
    const float ys = tsum4(u2f4(y[0]) + (lane < 8 ? u2f4(y[1]) : f32x4{0.f, 0.f, 0.f, 0.f}));
    if (c16 < 8) *(LAS float*)(L + fw) = ys;
    bar();
    if (w == 0) {
      const f32x4 t = *(LAS f32x4*)(L + fr);
      const float yt = rb(x32_sum((t.x + t.y) + (t.z + t.w)));
      __builtin_amdgcn_raw_buffer_store_b16((u16)pk2(hres + yt, 0.f), rout, oo, 0, 0);
      if (job == 0) st64(v2u{tag, tagh}, rws, lane == 0 ? WS_EPOCH : OOB);
    }
  }
  }
}

extern "C" void kernel_launch(void* const* d_in, const int* in_sizes, int n_in, void* d_out, int out_size, void* d_ws, size_t ws_size, hipStream_t stream) {
  fused_decode_block<<<256, 512, 0, stream>>>((const u16*)d_in[0], (const u16*)d_in[1], (const u16*)d_in[2], (const u16*)d_in[3], (unsigned char*)d_ws,
                                             (const u16*)d_in[4], (const u16*)d_in[6], (const u16*)d_in[7], (u16*)d_out);
}
```
